```python
import torch

def _softmax(x, axis=-1):
    return torch.softmax(x, dim=axis)
def _gelu(x):
    return torch.nn.functional.gelu(x, approximate="tanh")
def _silu(x):
    return torch.nn.functional.silu(x)
def _sigmoid(x):
    return torch.sigmoid(x)
def _layernorm(x, axis=-1, eps=1e-5):
    m = x.mean(dim=axis, keepdim=True)
    v = x.var(dim=axis, keepdim=True, unbiased=False)
    return (x - m) / torch.sqrt(v + eps)
def _scatter_add(tgt, idx, src, axis=0):
    return tgt.clone().index_add_(axis, idx, src)
def _segment_sum(src, seg, n):
    out = torch.zeros((n,) + src.shape[1:], dtype=src.dtype)
    return out.index_add_(0, seg, src)
def _pad(x, pw):
    flat = tuple(v for pair in reversed(pw) for v in pair)
    return torch.nn.functional.pad(x, flat)

kernel_name = 'standalone__mha_softmax__sampling'

def setup_inputs():

    _w = (lambda a: (lambda t: t.to(torch.bfloat16) if t.is_floating_point() else t)(torch.as_tensor(a)))
    B, H, Sq, Sk, D = 16, 32, 1, 2048, 128
    return {
        "q": _w((lambda *s: torch.randn(*s, dtype=torch.bfloat16) * 0.8)(B, H, Sq, D)),
        "k": _w((lambda *s: torch.randn(*s, dtype=torch.bfloat16) * 0.8)(B, H, Sk, D)),
        "v": _w((lambda *s: torch.randn(*s, dtype=torch.bfloat16) * 0.05)(B, H, Sk, D)),
    }

def reference(q, k, v):
    scale = q.shape[-1] ** -0.5
    scores = torch.einsum("bhqd,bhkd->bhqk", q, k) * scale
    p = _softmax(scores, axis=-1)
    return torch.einsum("bhqk,bhkd->bhqd", p, v)
```

```cpp
#include <hip/hip_runtime.h>
#include <hip/hip_bf16.h>
#include <stdint.h>

#define WAVES 8
#define TL 4
#define KV_NT 1

constexpr int D = 128, SK = 2048, BH = 512;
constexpr int NTHR = WAVES * 64;
constexpr int TKEYS = TL * 4;
constexpr int NT = SK / (TKEYS * WAVES);
static_assert(NT % 2 == 0, "even tile count");

using bf16x2 = __attribute__((ext_vector_type(2))) __bf16;
using u32x4 = __attribute__((ext_vector_type(4))) unsigned;
using f32x4 = __attribute__((ext_vector_type(4))) float;
using f32x2 = __attribute__((ext_vector_type(2))) float;

__device__ __forceinline__ float dot2(unsigned k, unsigned q, float acc) {
  return __builtin_amdgcn_fdot2_f32_bf16(__builtin_bit_cast(bf16x2, k), __builtin_bit_cast(bf16x2, q), acc, false);
}
template <int CTRL> __device__ __forceinline__ float dpp(float x) {
  return __builtin_bit_cast(float, __builtin_amdgcn_mov_dpp(__builtin_bit_cast(int, x), CTRL, 0xf, 0xf, true));
}
constexpr int XOR1 = 0xB1, XOR2 = 0x4E, HMIR = 0x141, MIR = 0x140;
__device__ __forceinline__ float row16_sum(float x) {
  x += dpp<XOR1>(x); x += dpp<XOR2>(x); x += dpp<HMIR>(x); x += dpp<MIR>(x); return x;
}
__device__ __forceinline__ float xrow_max(float x) {
  auto s = __builtin_amdgcn_permlane16_swap(__float_as_uint(x), __float_as_uint(x), false, false);
  x = fmaxf(__uint_as_float(s[0]), __uint_as_float(s[1]));
  auto t = __builtin_amdgcn_permlane32_swap(__float_as_uint(x), __float_as_uint(x), false, false);
  return fmaxf(__uint_as_float(t[0]), __uint_as_float(t[1]));
}
__device__ __forceinline__ float xrow_sum(float x) {
  auto s = __builtin_amdgcn_permlane16_swap(__float_as_uint(x), __float_as_uint(x), false, false);
  x = __uint_as_float(s[0]) + __uint_as_float(s[1]);
  auto t = __builtin_amdgcn_permlane32_swap(__float_as_uint(x), __float_as_uint(x), false, false);
  return __uint_as_float(t[0]) + __uint_as_float(t[1]);
}
__device__ __forceinline__ u32x4 ldkv(const u32x4* p) {
  return __builtin_nontemporal_load(p);
}

struct State { float m, l; float acc[8]; };

__device__ __forceinline__ void tile_compute(const u32x4 (&K)[TL], const u32x4 (&V)[TL], const u32x4& q, State& st) {
  constexpr float C2 = 0.088388347648318440f * 1.4426950408889634f;
  float s[TL];
#pragma unroll
  for (int i = 0; i < TL; ++i) {
    float d = dot2(K[i][0], q[0], 0.f);
    d = dot2(K[i][1], q[1], d); d = dot2(K[i][2], q[2], d); d = dot2(K[i][3], q[3], d);
    s[i] = row16_sum(d) * C2;
  }
  float tm = s[0];
#pragma unroll
  for (int i = 1; i < TL; ++i) tm = fmaxf(tm, s[i]);
  const float mn = fmaxf(st.m, tm);
  const float alpha = __builtin_amdgcn_exp2f(st.m - mn);
  st.m = mn;
  st.l *= alpha;
#pragma unroll
  for (int k = 0; k < 8; ++k) st.acc[k] *= alpha;
#pragma unroll
  for (int i = 0; i < TL; ++i) {
    const float p = __builtin_amdgcn_exp2f(s[i] - mn);
    st.l += p;
#pragma unroll
    for (int k = 0; k < 4; ++k) {
      st.acc[2 * k] = fmaf(p, __uint_as_float(V[i][k] << 16), st.acc[2 * k]);
      st.acc[2 * k + 1] = fmaf(p, __uint_as_float(V[i][k] & 0xffff0000u), st.acc[2 * k + 1]);
    }
  }
}

__global__ void __launch_bounds__(NTHR) attn_decode(const u32x4* __restrict__ q, const u32x4* __restrict__ k,
                                                    const u32x4* __restrict__ v, __hip_bfloat16* __restrict__ out) {
  __shared__ __attribute__((aligned(16))) float lds[WAVES * (D + 4)];
  const int bh = blockIdx.x;
  const int tid = threadIdx.x, lane = tid & 63, wave = __builtin_amdgcn_readfirstlane(tid >> 6);
  const int c = lane & 15, r = lane >> 4;
  const u32x4* kp = k + (size_t)bh * (SK * 16) + wave * (TL * 64) + lane;
  const u32x4* vp = v + (size_t)bh * (SK * 16) + wave * (TL * 64) + lane;
  constexpr int STEP = WAVES * TL * 64;
  const u32x4 qv = q[bh * 16 + c];
  u32x4 KA[TL], VA[TL], KB[TL], VB[TL];
#pragma unroll
  for (int i = 0; i < TL; ++i) KA[i] = ldkv(kp + i * 64);
#pragma unroll
  for (int i = 0; i < TL; ++i) VA[i] = ldkv(vp + i * 64);
  State st; st.m = -1e30f; st.l = 0.f;
#pragma unroll
  for (int j = 0; j < 8; ++j) st.acc[j] = 0.f;
  for (int t = 0; t < NT; t += 2) {
    {
      const u32x4* kn = kp + (t + 1) * STEP; const u32x4* vn = vp + (t + 1) * STEP;
#pragma unroll
      for (int i = 0; i < TL; ++i) KB[i] = ldkv(kn + i * 64);
#pragma unroll
      for (int i = 0; i < TL; ++i) VB[i] = ldkv(vn + i * 64);
    }
    tile_compute(KA, VA, qv, st);
    if (t + 2 < NT) {
      const u32x4* kn = kp + (t + 2) * STEP; const u32x4* vn = vp + (t + 2) * STEP;
#pragma unroll
      for (int i = 0; i < TL; ++i) KA[i] = ldkv(kn + i * 64);
#pragma unroll
      for (int i = 0; i < TL; ++i) VA[i] = ldkv(vn + i * 64);
    }
    tile_compute(KB, VB, qv, st);
  }
  const float M = xrow_max(st.m);
  const float w = __builtin_amdgcn_exp2f(st.m - M);
  const float L = xrow_sum(st.l * w);
  float o[8];
#pragma unroll
  for (int j = 0; j < 8; ++j) o[j] = xrow_sum(st.acc[j] * w);
  float* rec = lds + wave * (D + 4);
  if (r == 0) {
    *reinterpret_cast<f32x4*>(rec + 4 + 8 * c) = f32x4{o[0], o[1], o[2], o[3]};
    *reinterpret_cast<f32x4*>(rec + 8 + 8 * c) = f32x4{o[4], o[5], o[6], o[7]};
    if (c == 0) { rec[0] = M; rec[1] = L; }
  }
  __syncthreads();
  if (wave == 0) {
    float MM = lds[0];
#pragma unroll
    for (int u = 1; u < WAVES; ++u) MM = fmaxf(MM, lds[u * (D + 4)]);
    float LL = 0.f, o0 = 0.f, o1 = 0.f;
#pragma unroll
    for (int u = 0; u < WAVES; ++u) {
      const float* rr = lds + u * (D + 4);
      const float wt = __builtin_amdgcn_exp2f(rr[0] - MM);
      const f32x2 a = *reinterpret_cast<const f32x2*>(rr + 4 + 2 * lane);
      LL = fmaf(wt, rr[1], LL); o0 = fmaf(wt, a[0], o0); o1 = fmaf(wt, a[1], o1);
    }
    const float inv = 1.f / LL;
    __hip_bfloat162 res; res.x = __float2bfloat16(o0 * inv); res.y = __float2bfloat16(o1 * inv);
    *reinterpret_cast<__hip_bfloat162*>(out + bh * D + 2 * lane) = res;
  }
}

extern "C" void kernel_launch(void* const* d_in, const int* in_sizes, int n_in, void* d_out, int out_size,
                              void* d_ws, size_t ws_size, hipStream_t stream) {
  attn_decode<<<BH, NTHR, 0, stream>>>((const u32x4*)d_in[0], (const u32x4*)d_in[1], (const u32x4*)d_in[2],
                                       (__hip_bfloat16*)d_out);
}
```

```cpp
#define KNT 1
#define VNT 1
#include <hip/hip_runtime.h>
#include <hip/hip_bf16.h>
#include <stdint.h>

#define WAVES 8
#define TL 4
#define KV_NT 1

constexpr int D = 128, SK = 2048, BH = 512;
constexpr int NTHR = WAVES * 64;
constexpr int TKEYS = TL * 4;
constexpr int NT = SK / (TKEYS * WAVES);
static_assert(NT % 2 == 0, "even tile count");

using bf16x2 = __attribute__((ext_vector_type(2))) __bf16;
using u32x4 = __attribute__((ext_vector_type(4))) unsigned;
using f32x4 = __attribute__((ext_vector_type(4))) float;
using f32x2 = __attribute__((ext_vector_type(2))) float;

__device__ __forceinline__ float dot2(unsigned k, unsigned q, float acc) {
  return __builtin_amdgcn_fdot2_f32_bf16(__builtin_bit_cast(bf16x2, k), __builtin_bit_cast(bf16x2, q), acc, false);
}
template <int CTRL> __device__ __forceinline__ float dpp(float x) {
  return __builtin_bit_cast(float, __builtin_amdgcn_mov_dpp(__builtin_bit_cast(int, x), CTRL, 0xf, 0xf, true));
}
constexpr int XOR1 = 0xB1, XOR2 = 0x4E, HMIR = 0x141, MIR = 0x140;
__device__ __forceinline__ float row16_sum(float x) {
  x += dpp<XOR1>(x); x += dpp<XOR2>(x); x += dpp<HMIR>(x); x += dpp<MIR>(x); return x;
}
__device__ __forceinline__ float xrow_max(float x) {
  auto s = __builtin_amdgcn_permlane16_swap(__float_as_uint(x), __float_as_uint(x), false, false);
  x = fmaxf(__uint_as_float(s[0]), __uint_as_float(s[1]));
  auto t = __builtin_amdgcn_permlane32_swap(__float_as_uint(x), __float_as_uint(x), false, false);
  return fmaxf(__uint_as_float(t[0]), __uint_as_float(t[1]));
}
__device__ __forceinline__ float xrow_sum(float x) {
  auto s = __builtin_amdgcn_permlane16_swap(__float_as_uint(x), __float_as_uint(x), false, false);
  x = __uint_as_float(s[0]) + __uint_as_float(s[1]);
  auto t = __builtin_amdgcn_permlane32_swap(__float_as_uint(x), __float_as_uint(x), false, false);
  return __uint_as_float(t[0]) + __uint_as_float(t[1]);
}
template <int NT_> __device__ __forceinline__ u32x4 ldp(const u32x4* p) {
  if constexpr (NT_) return __builtin_nontemporal_load(p); else return *p;
}

struct State { float m, l; float acc[8]; };

__device__ __forceinline__ void tile_compute(const u32x4 (&K)[TL], const u32x4 (&V)[TL], const u32x4& q, State& st) {
  constexpr float C2 = 0.088388347648318440f * 1.4426950408889634f;
  float s[TL];
#pragma unroll
  for (int i = 0; i < TL; ++i) {
    float d = dot2(K[i][0], q[0], 0.f);
    d = dot2(K[i][1], q[1], d); d = dot2(K[i][2], q[2], d); d = dot2(K[i][3], q[3], d);
    s[i] = row16_sum(d) * C2;
  }
  float tm = s[0];
#pragma unroll
  for (int i = 1; i < TL; ++i) tm = fmaxf(tm, s[i]);
  const float mn = fmaxf(st.m, tm);
  const float alpha = __builtin_amdgcn_exp2f(st.m - mn);
  st.m = mn;
  st.l *= alpha;
#pragma unroll
  for (int k = 0; k < 8; ++k) st.acc[k] *= alpha;
#pragma unroll
  for (int i = 0; i < TL; ++i) {
    const float p = __builtin_amdgcn_exp2f(s[i] - mn);
    st.l += p;
#pragma unroll
    for (int k = 0; k < 4; ++k) {
      st.acc[2 * k] = fmaf(p, __uint_as_float(V[i][k] << 16), st.acc[2 * k]);
      st.acc[2 * k + 1] = fmaf(p, __uint_as_float(V[i][k] & 0xffff0000u), st.acc[2 * k + 1]);
    }
  }
}

__global__ void __launch_bounds__(NTHR) attn_decode(const u32x4* __restrict__ q, const u32x4* __restrict__ k,
                                                    const u32x4* __restrict__ v, __hip_bfloat16* __restrict__ out) {
  __shared__ __attribute__((aligned(16))) float lds[WAVES * (D + 4)];
  const int bh = blockIdx.x;
  const int tid = threadIdx.x, lane = tid & 63, wave = __builtin_amdgcn_readfirstlane(tid >> 6);
  const int c = lane & 15, r = lane >> 4;
  const u32x4* kp = k + (size_t)bh * (SK * 16) + wave * (TL * 64) + lane;
  const u32x4* vp = v + (size_t)bh * (SK * 16) + wave * (TL * 64) + lane;
  constexpr int STEP = WAVES * TL * 64;
  const u32x4 qv = q[bh * 16 + c];
  u32x4 KA[TL], VA[TL], KB[TL], VB[TL];
#pragma unroll
  for (int i = 0; i < TL; ++i) KA[i] = ldp<KNT>(kp + i * 64);
#pragma unroll
  for (int i = 0; i < TL; ++i) VA[i] = ldp<VNT>(vp + i * 64);
  State st; st.m = -1e30f; st.l = 0.f;
#pragma unroll
  for (int j = 0; j < 8; ++j) st.acc[j] = 0.f;
  for (int t = 0; t < NT; t += 2) {
    {
      const u32x4* kn = kp + (t + 1) * STEP; const u32x4* vn = vp + (t + 1) * STEP;
#pragma unroll
      for (int i = 0; i < TL; ++i) KB[i] = ldp<KNT>(kn + i * 64);
#pragma unroll
      for (int i = 0; i < TL; ++i) VB[i] = ldp<VNT>(vn + i * 64);
    }
    tile_compute(KA, VA, qv, st);
    if (t + 2 < NT) {
      const u32x4* kn = kp + (t + 2) * STEP; const u32x4* vn = vp + (t + 2) * STEP;
#pragma unroll
      for (int i = 0; i < TL; ++i) KA[i] = ldp<KNT>(kn + i * 64);
#pragma unroll
      for (int i = 0; i < TL; ++i) VA[i] = ldp<VNT>(vn + i * 64);
    }
    tile_compute(KB, VB, qv, st);
  }
  asm volatile(".rept 512\n s_nop 0\n .endr" ::: "memory");
  const float M = xrow_max(st.m);
  const float w = __builtin_amdgcn_exp2f(st.m - M);
  const float L = xrow_sum(st.l * w);
  float o[8];
#pragma unroll
  for (int j = 0; j < 8; ++j) o[j] = xrow_sum(st.acc[j] * w);
  float* rec = lds + wave * (D + 4);
  if (r == 0) {
    *reinterpret_cast<f32x4*>(rec + 4 + 8 * c) = f32x4{o[0], o[1], o[2], o[3]};
    *reinterpret_cast<f32x4*>(rec + 8 + 8 * c) = f32x4{o[4], o[5], o[6], o[7]};
    if (c == 0) { rec[0] = M; rec[1] = L; }
  }
  __syncthreads();
  if (wave == 0) {
    float MM = lds[0];
#pragma unroll
    for (int u = 1; u < WAVES; ++u) MM = fmaxf(MM, lds[u * (D + 4)]);
    float LL = 0.f, o0 = 0.f, o1 = 0.f;
#pragma unroll
    for (int u = 0; u < WAVES; ++u) {
      const float* rr = lds + u * (D + 4);
      const float wt = __builtin_amdgcn_exp2f(rr[0] - MM);
      const f32x2 a = *reinterpret_cast<const f32x2*>(rr + 4 + 2 * lane);
      LL = fmaf(wt, rr[1], LL); o0 = fmaf(wt, a[0], o0); o1 = fmaf(wt, a[1], o1);
    }
    const float inv = 1.f / LL;
    __hip_bfloat162 res; res.x = __float2bfloat16(o0 * inv); res.y = __float2bfloat16(o1 * inv);
    *reinterpret_cast<__hip_bfloat162*>(out + bh * D + 2 * lane) = res;
  }
}

extern "C" void kernel_launch(void* const* d_in, const int* in_sizes, int n_in, void* d_out, int out_size,
                              void* d_ws, size_t ws_size, hipStream_t stream) {
  attn_decode<<<BH, NTHR, 0, stream>>>((const u32x4*)d_in[0], (const u32x4*)d_in[1], (const u32x4*)d_in[2],
                                       (__hip_bfloat16*)d_out);
}
```

```cpp
#include <hip/hip_runtime.h>
#include <hip/hip_bf16.h>
#include <stdint.h>

#define PACE_T 6800
#define PACE_A 20
#define CODEPF 16000
constexpr int PACE_C = 2;
constexpr int GAP = 25;
constexpr int SLIP = 240, NLATE = 40, NOK = 48;

constexpr int D = 128, SK = 2048, BH = 512, HPW = 2, WAVES = 4, RING = 16, NTHR = WAVES * 64;
constexpr int GPH = SK / (64 * WAVES);
constexpr int NG = GPH * HPW;
constexpr int NLD = 2 * NG * 16;
constexpr int HEADB = SK * D * 2;
constexpr int GBYTES = 65536;
constexpr int NT = 2, WT = 19;

constexpr float C2 = 0.088388347648318440f * 1.4426950408889634f;

using bf16x2 = __attribute__((ext_vector_type(2))) __bf16;
using u32x4 = __attribute__((ext_vector_type(4))) unsigned;
using f32x4 = __attribute__((ext_vector_type(4))) float;
using f32x2 = __attribute__((ext_vector_type(2))) float;
using rsrc_t = __amdgpu_buffer_rsrc_t;

__device__ __forceinline__ float dot2(unsigned k, unsigned q, float acc) {
  return __builtin_amdgcn_fdot2_f32_bf16(__builtin_bit_cast(bf16x2, k), __builtin_bit_cast(bf16x2, q), acc, false);
}
template <int CTRL> __device__ __forceinline__ float dpp(float x) {
  return __builtin_bit_cast(float, __builtin_amdgcn_mov_dpp(__builtin_bit_cast(int, x), CTRL, 0xf, 0xf, true));
}
constexpr int XOR1 = 0xB1, XOR2 = 0x4E, XOR7 = 0x141, XOR8 = 0x128, XOR15 = 0x140;
template <int CTRL> __device__ __forceinline__ float comb(float a, float b, bool hi) {
  const float keep = hi ? b : a, give = hi ? a : b;
  return keep + dpp<CTRL>(give);
}
__device__ __forceinline__ float wave_max(float x) {
  x = fmaxf(x, dpp<XOR1>(x)); x = fmaxf(x, dpp<XOR2>(x)); x = fmaxf(x, dpp<XOR7>(x)); x = fmaxf(x, dpp<XOR15>(x));
  auto s = __builtin_amdgcn_permlane16_swap(__float_as_uint(x), __float_as_uint(x), false, false);
  x = fmaxf(__uint_as_float(s[0]), __uint_as_float(s[1]));
  auto t = __builtin_amdgcn_permlane32_swap(__float_as_uint(x), __float_as_uint(x), false, false);
  return fmaxf(__uint_as_float(t[0]), __uint_as_float(t[1]));
}
__device__ __forceinline__ float wave_sum(float x) {
  x += dpp<XOR1>(x); x += dpp<XOR2>(x); x += dpp<XOR7>(x); x += dpp<XOR15>(x);
  auto s = __builtin_amdgcn_permlane16_swap(__float_as_uint(x), __float_as_uint(x), false, false);
  x = __uint_as_float(s[0]) + __uint_as_float(s[1]);
  auto t = __builtin_amdgcn_permlane32_swap(__float_as_uint(x), __float_as_uint(x), false, false);
  return __uint_as_float(t[0]) + __uint_as_float(t[1]);
}
constexpr int brev4(int i) { return ((i & 1) << 3) | ((i & 2) << 1) | ((i & 4) >> 1) | ((i & 8) >> 3); }
constexpr int ldoff(int i) { return (i & 7) * 1024 + (i >> 3) * 16384; }
#define SB() __builtin_amdgcn_sched_barrier(0)
#define LDS_BARRIER() asm volatile("s_waitcnt lgkmcnt(0)\n\ts_barrier" ::: "memory")
__device__ __forceinline__ u32x4 bld(rsrc_t rs, int voff, int soff, int coff) {
  return __builtin_amdgcn_raw_buffer_load_b128(rs, voff + (coff & 4095), soff + (coff & ~4095), NT);
}

struct Pacer { unsigned t0; int sum, prev, lim; };
__device__ __forceinline__ int since(unsigned t0) { return (int)((unsigned)__builtin_amdgcn_s_memrealtime() - t0); }
__device__ __forceinline__ int outstanding() { return (int)__builtin_amdgcn_s_getreg(7 | (3 << 11)); }
__device__ __forceinline__ void pace(Pacer& p, int ld) {
  const int tgt = PACE_A + (int)((unsigned)(ld - PACE_C) * PACE_T / NLD);
  int now = since(p.t0);
  const int late = now - tgt;
  if (late >= p.lim) {
    const int d = max(SLIP, late);
    p.t0 += d; now -= d; p.lim = 1 << 20; p.sum = p.prev = 1000;
  }
  p.sum += outstanding();
  while (now < tgt) { __builtin_amdgcn_s_sleep(1); now = since(p.t0); }
}
__device__ __forceinline__ void group_done(Pacer& p) {
  p.lim = min(p.prev, p.sum) <= NOK ? NLATE : 1 << 20; p.prev = p.sum; p.sum = 0;
}

constexpr int LDS_S = 0;
constexpr int LDS_A = LDS_S + WAVES * NG * 64;
constexpr int LDS_ML = LDS_A + HPW * WAVES * 4 * D;
constexpr int LDS_N = LDS_ML + HPW * WAVES * 2;

__device__ __forceinline__ float k_group(u32x4 (&R)[RING], rsrc_t nr, int nxt, int voff, const u32x4& qv,
                                         bool b8, bool b4, bool b2, bool b1, Pacer& tp, int ld0) {
  float x[16];
#pragma unroll
  for (int i = 0; i < 16; ++i) {
    const u32x4 kk = R[i];
    float d = dot2(kk[0], qv[0], 0.f);
    d = dot2(kk[1], qv[1], d); d = dot2(kk[2], qv[2], d); d = dot2(kk[3], qv[3], d);
    asm volatile("" : "+v"(d) :: "memory");
    if (i % 2 == 0) pace(tp, ld0 + i);
    SB();
    R[i] = bld(nr, voff, nxt, ldoff(i));
    SB();
    x[i] = d;
    if (i & 1) x[i] = comb<XOR8>(x[i - 1], x[i], b8);
    if ((i & 3) == 3) x[i] = comb<XOR7>(x[i - 2], x[i], b4);
    if ((i & 7) == 7) x[i] = comb<XOR2>(x[i - 4], x[i], b2);
    if (i == 15) x[i] = comb<XOR1>(x[i - 8], x[i], b1);
  }
  return x[15];
}

template <bool LAST> __device__ __forceinline__ void v_group(u32x4 (&R)[RING], rsrc_t nr, int nxt, int voff, float p, float (&acc)[8], Pacer& tp, int ld0) {
#pragma unroll
  for (int i = 0; i < 16; ++i) {
    const u32x4 x = R[i];
    float pb;
    switch (brev4(i)) {
#define C_(n) case n: pb = dpp<0x150 + n>(p); break;
      C_(0) C_(1) C_(2) C_(3) C_(4) C_(5) C_(6) C_(7) C_(8) C_(9) C_(10) C_(11) C_(12) C_(13) C_(14) default: pb = dpp<0x15f>(p); break;
#undef C_
    }
#pragma unroll
    for (int k = 0; k < 4; ++k) {
      acc[2 * k] = fmaf(pb, __uint_as_float(x[k] << 16), acc[2 * k]);
      acc[2 * k + 1] = fmaf(pb, __uint_as_float(x[k] & 0xffff0000u), acc[2 * k + 1]);
    }
    asm volatile("" : "+v"(acc[0]), "+v"(acc[1]), "+v"(acc[2]), "+v"(acc[3]), "+v"(acc[4]), "+v"(acc[5]), "+v"(acc[6]), "+v"(acc[7]) :: "memory");
    if (!LAST) {
      if (i % 2 == 0) pace(tp, ld0 + i);
      SB();
      R[i] = bld(nr, voff, nxt, ldoff(i));
    }
    SB();
  }
}

__device__ __forceinline__ void merge_weights(const float* ml, float (&wt)[WAVES]) {
  const f32x4 a = *reinterpret_cast<const f32x4*>(ml), b = *reinterpret_cast<const f32x4*>(ml + 4);
  const float M = fmaxf(fmaxf(a[0], a[2]), fmaxf(b[0], b[2]));
  wt[0] = __builtin_amdgcn_exp2f((a[0] - M) * C2); wt[1] = __builtin_amdgcn_exp2f((a[2] - M) * C2);
  wt[2] = __builtin_amdgcn_exp2f((b[0] - M) * C2); wt[3] = __builtin_amdgcn_exp2f((b[2] - M) * C2);
  const float inv = 1.f / fmaf(wt[3], b[3], fmaf(wt[2], b[1], fmaf(wt[1], a[3], wt[0] * a[1])));
#pragma unroll
  for (int u = 0; u < WAVES; ++u) wt[u] *= inv;
}
__device__ __forceinline__ void finish_head(const float* acc, const float (&wt)[WAVES], int lane, rsrc_t orr, int bh) {
  f32x2 a[WAVES * 4];
#pragma unroll
  for (int u = 0; u < WAVES * 4; ++u) a[u] = *reinterpret_cast<const f32x2*>(acc + u * D + 2 * lane);
  SB();
  f32x2 o = wt[0] * ((a[0] + a[1]) + (a[2] + a[3]));
#pragma unroll
  for (int u = 1; u < WAVES; ++u) o = __builtin_elementwise_fma(f32x2{wt[u], wt[u]}, (a[4 * u] + a[4 * u + 1]) + (a[4 * u + 2] + a[4 * u + 3]), o);
  __hip_bfloat162 res; res.x = __float2bfloat16(o[0]); res.y = __float2bfloat16(o[1]);
  __builtin_amdgcn_raw_buffer_store_b32(*reinterpret_cast<unsigned*>(&res), orr, lane * 4, bh * (D * 2), WT);
}

__global__ void __launch_bounds__(NTHR, 1) attn_decode(const u32x4* __restrict__ q, const u32x4* __restrict__ k,
                                                       const u32x4* __restrict__ v, __hip_bfloat16* __restrict__ out) {
  __shared__ __attribute__((aligned(16))) float lds[LDS_N];
  const int wg = blockIdx.x ^ ((__builtin_popcount(blockIdx.x >> 3) & 1) << 2);
  const int bh0 = wg * HPW;
  const int tid = threadIdx.x, lane = tid & 63, wave = __builtin_amdgcn_readfirstlane(tid >> 6);
  const int c = lane & 15, r = lane >> 4;
  const rsrc_t cr = __builtin_amdgcn_make_buffer_rsrc((void*)(__builtin_amdgcn_s_getpc() & ~127ull), 0, CODEPF, 0x00020000);
  const int voff = lane * 16 + ((tid >> 6) & 1) * 8192 + (tid >> 7) * 32768;
  const unsigned stag = (((wave + 4 * (blockIdx.x >> 3)) & 7) * 2 * PACE_T) / (NLD * 8);
  const int woff = wave * (HEADB / 2), coff = tid * 128, qoff = (bh0 * 16 + c) * 16, ooff = bh0 * (D * 2);
  const size_t hoff = (size_t)bh0 * HEADB;
  asm volatile("" :: "s"(cr), "s"(woff), "v"(voff), "v"(coff), "s"(stag), "v"(qoff), "s"(ooff), "s"(hoff));
  SB();
  const rsrc_t kr = __builtin_amdgcn_make_buffer_rsrc((void*)((const char*)k + hoff), 0, HPW * HEADB, 0x00020000);
  const rsrc_t vr = __builtin_amdgcn_make_buffer_rsrc((void*)((const char*)v + hoff), 0, HPW * HEADB, 0x00020000);
  const rsrc_t qr = __builtin_amdgcn_make_buffer_rsrc((void*)q, 0, BH * D * 2, 0x00020000);
  const rsrc_t orr = __builtin_amdgcn_make_buffer_rsrc((void*)out, 0, BH * D * 2, 0x00020000);
  Pacer tp; tp.sum = 0; tp.prev = 1000; tp.lim = 1 << 20;
  unsigned warm[4];
  u32x4 qv[HPW], R[RING];
  unsigned long long tk;
  unsigned pk, pv;
  asm volatile("s_load_dword %0, %2, 0x0\n\ts_load_dword %1, %3, 0x0" : "=&s"(pk), "=&s"(pv) : "s"(k), "s"(v) : "memory");
  SB();
#pragma unroll
  for (int i = 0; i < RING; ++i) {
    if (i == PACE_C) {
      warm[3] = __builtin_amdgcn_raw_buffer_load_b32(cr, coff, 0, 0);
#pragma unroll
      for (int h = 0; h < HPW; ++h) qv[h] = __builtin_amdgcn_raw_buffer_load_b128(qr, qoff + h * 256, 0, 0);
      SB();
      warm[0] = __builtin_amdgcn_raw_buffer_load_b32(vr, 0, woff, NT);
      warm[1] = __builtin_amdgcn_raw_buffer_load_b32(kr, 0, woff + HEADB / 2 - 128, NT);
      warm[2] = __builtin_amdgcn_raw_buffer_load_b32(orr, 0, ooff, NT);
      SB();
      asm volatile("s_waitcnt lgkmcnt(0) ; probes in %1 %2\n\ts_memrealtime %0\n\ts_waitcnt lgkmcnt(0)" : "=&s"(tk), "+s"(pk), "+s"(pv) :: "memory");
      tp.t0 = (unsigned)tk + stag;
    }
    if (i >= PACE_C && i % 2 == 0) pace(tp, i);
    SB(); R[i] = bld(kr, voff, 0, ldoff(i)); SB();
  }
  asm volatile("" :: "v"(warm[0]), "v"(warm[1]), "v"(warm[2]), "v"(warm[3]));
  tp.sum = 1000;
  const bool b8 = c & 8, b4 = c & 4, b2 = c & 2, b1 = c & 1;
  float* sw = lds + LDS_S + wave * (NG * 64) + lane;
  float mh[HPW];
#pragma unroll
  for (int h = 0; h < HPW; ++h) {
    float mx = -INFINITY;
    const int gend = (h == HPW - 1) ? GPH - 1 : GPH;
    for (int gg = 0; gg < gend; ++gg) {
      const int g = h * GPH + gg;
      const float s = k_group(R, kr, (g + 1) * GBYTES, voff, qv[h], b8, b4, b2, b1, tp, (g + 1) * 16);
      sw[g * 64] = s;
      mx = fmaxf(mx, s);
      group_done(tp);
    }
    if (h == HPW - 1) {
      tp.t0 += GAP;
      const float s = k_group(R, vr, 0, voff, qv[h], b8, b4, b2, b1, tp, NG * 16);
      sw[(NG - 1) * 64] = s;
      mx = fmaxf(mx, s);
      group_done(tp);
    }
    mh[h] = wave_max(mx);
  }
  float wt[WAVES];
#pragma unroll
  for (int h = 0; h < HPW; ++h) {
    float acc[8], l = 0.f;
#pragma unroll
    for (int j = 0; j < 8; ++j) acc[j] = 0.f;
    const int gend = (h == HPW - 1) ? GPH - 1 : GPH;
    for (int gg = 0; gg < gend; ++gg) {
      const int g = h * GPH + gg;
      const float p = __builtin_amdgcn_exp2f((sw[g * 64] - mh[h]) * C2);
      l += p;
      v_group<false>(R, vr, (g + 1) * GBYTES, voff, p, acc, tp, (NG + g + 1) * 16);
      group_done(tp);
    }
    float p = 0.f;
    if (h == HPW - 1) { p = __builtin_amdgcn_exp2f((sw[(NG - 1) * 64] - mh[h]) * C2); l += p; }
    l = wave_sum(l);
    if (lane == 0) *reinterpret_cast<f32x2*>(lds + LDS_ML + (h * WAVES + wave) * 2) = f32x2{mh[h], l};
    if (h == HPW - 1) {
      LDS_BARRIER();
      merge_weights(lds + LDS_ML + (wave & 1) * (WAVES * 2), wt);
      asm volatile("" : "+v"(wt[0]), "+v"(wt[1]), "+v"(wt[2]), "+v"(wt[3]));
      SB();
      v_group<true>(R, vr, 0, voff, p, acc, tp, 0);
    }
    float* a = lds + LDS_A + ((h * WAVES + wave) * 4 + r) * D + 8 * c;
    *reinterpret_cast<f32x4*>(a) = f32x4{acc[0], acc[1], acc[2], acc[3]};
    *reinterpret_cast<f32x4*>(a + 4) = f32x4{acc[4], acc[5], acc[6], acc[7]};
  }
  LDS_BARRIER();
  if (wave < HPW) {
    finish_head(lds + LDS_A + wave * (WAVES * 4 * D), wt, lane, orr, bh0 + wave);
  }
}

extern "C" void kernel_launch(void* const* d_in, const int* in_sizes, int n_in, void* d_out, int out_size,
                              void* d_ws, size_t ws_size, hipStream_t stream) {
  attn_decode<<<BH / HPW, NTHR, 0, stream>>>((const u32x4*)d_in[0], (const u32x4*)d_in[1], (const u32x4*)d_in[2],
                                             (__hip_bfloat16*)d_out);
}
```
